```python
import torch

def _softmax(x, axis=-1):
    return torch.softmax(x, dim=axis)
def _gelu(x):
    return torch.nn.functional.gelu(x, approximate="tanh")
def _silu(x):
    return torch.nn.functional.silu(x)
def _sigmoid(x):
    return torch.sigmoid(x)
def _layernorm(x, axis=-1, eps=1e-5):
    m = x.mean(dim=axis, keepdim=True)
    v = x.var(dim=axis, keepdim=True, unbiased=False)
    return (x - m) / torch.sqrt(v + eps)
def _scatter_add(tgt, idx, src, axis=0):
    return tgt.clone().index_add_(axis, idx, src)
def _segment_sum(src, seg, n):
    out = torch.zeros((n,) + src.shape[1:], dtype=src.dtype)
    return out.index_add_(0, seg, src)
def _pad(x, pw):
    flat = tuple(v for pair in reversed(pw) for v in pair)
    return torch.nn.functional.pad(x, flat)

kernel_name = 'gated_mix__mha_block__moe_arch_mod__prompt'

def setup_inputs():

    _w = (lambda a: (lambda t: t.to(torch.bfloat16) if t.is_floating_point() else t)(torch.as_tensor(a)))
    def _A_setup():
        B, S, D, H = 1, 2048, 512, 32
        return {
            "x": _w((lambda *s: torch.randn(*s, dtype=torch.bfloat16) * 0.05)(B, S, D)),
            "wqkv": _w((lambda *s: torch.randn(*s, dtype=torch.bfloat16) * 0.8)(D, 3 * D)),
            "wo": _w((lambda *s: torch.randn(*s, dtype=torch.bfloat16) * 0.1)(D, D)),
        }
    _A_in = _A_setup()

    def _B_setup():
        B, S, D, F, C = 1, 2048, 512, 1376, 1
        return {
            "x": _w((lambda *s: torch.randn(*s, dtype=torch.bfloat16) * 0.05)(B, S, D)),
            "w_r": _w((lambda *s: torch.randn(*s, dtype=torch.bfloat16) * 3.2)(D)),
            "w_gate": _w((lambda *s: torch.randn(*s, dtype=torch.bfloat16) * 0.2)(D, F)),
            "w_up": _w((lambda *s: torch.randn(*s, dtype=torch.bfloat16) * 0.2)(D, F)),
            "w_down": _w((lambda *s: torch.randn(*s, dtype=torch.bfloat16) * 0.2)(F, D)),
        }
    _B_in = _B_setup()

    return {
        "A_x": _A_in["x"],
        "A_wqkv": _A_in["wqkv"],
        "A_wo": _A_in["wo"],
        "B_x": _B_in["x"],
        "B_w_r": _B_in["w_r"],
        "B_w_gate": _B_in["w_gate"],
        "B_w_up": _B_in["w_up"],
        "B_w_down": _B_in["w_down"],
        "gate_w": _w((lambda *s: torch.randn(*s, dtype=torch.bfloat16) * 0.05)(512) * 16.0),
    }

def reference(A_x, A_wqkv, A_wo, B_x, B_w_r, B_w_gate, B_w_up, B_w_down, gate_w):
    def _A_ref(x, wqkv, wo):
        B, S, D = x.shape
        H = 32
        Dh = D // H
        qkv = (lambda x, *d: x.permute(*d))((x @ wqkv).reshape(B, S, 3, H, Dh), 2, 0, 3, 1, 4)
        q, k, v = qkv[0], qkv[1], qkv[2]
        scores = torch.einsum("bhqd,bhkd->bhqk", q, k) * (Dh ** -0.5)
        p = _softmax(scores, axis=-1)
        o = torch.einsum("bhqk,bhkd->bhqd", p, v)
        return (lambda x, *d: x.permute(*d))(o, 0, 2, 1, 3).reshape(B, S, D) @ wo

    def _B_ref(x, w_r, w_gate, w_up, w_down):
        B, S, D = x.shape
        C = 1
        r = x @ w_r
        idx = torch.argsort(-r, axis=-1)[:, :C]
        idx_e = (lambda x, n, axis: torch.repeat_interleave(x, n, dim=axis))(idx.reshape(B, C, 1), D, axis=2)
        x_sel = (lambda x, i, axis: torch.take_along_dim(x, i, dim=axis))(x, idx_e, axis=1)
        g = _silu(x_sel @ w_gate)
        u = x_sel @ w_up
        y = (g * u) @ w_down
        r_sel = _sigmoid((lambda x, i, axis: torch.take_along_dim(x, i, dim=axis))(r, idx, axis=1))
        y = y * r_sel.reshape(B, C, 1)
        b_off = (torch.arange(B) * S).reshape(B, 1)
        flat_idx = (idx + b_off).reshape(B * C)
        zero = (x * 0.0).reshape(B * S, D)
        upd = _scatter_add(zero, flat_idx, y.reshape(B * C, D), axis=0)
        return x + upd.reshape(B, S, D)

    x = A_x
    g = _sigmoid((x * gate_w).sum(-1, keepdims=True))
    a = _A_ref(x, A_wqkv, A_wo)
    b = _B_ref(x, B_w_r, B_w_gate, B_w_up, B_w_down)
    return g * a + (1 - g) * b
```

```cpp
#include <hip/hip_runtime.h>
#include <hip/hip_bf16.h>
#include <stdint.h>

#define LAS __attribute__((address_space(3)))
#define GAS __attribute__((address_space(1)))
typedef __attribute__((ext_vector_type(8))) __bf16 bf16x8;
typedef __bf16 bf16x2 __attribute__((ext_vector_type(2)));
typedef short s16x4 __attribute__((ext_vector_type(4)));
typedef float f32x2 __attribute__((ext_vector_type(2)));
typedef float f32x4 __attribute__((ext_vector_type(4)));
typedef float f32x16 __attribute__((ext_vector_type(16)));
typedef unsigned u32x4 __attribute__((ext_vector_type(4)));
typedef unsigned u32x2 __attribute__((ext_vector_type(2)));
typedef unsigned long long u64;
typedef GAS u64 gu64;

constexpr int DM = 512, FF = 1376, NSL = 172;
constexpr float QSCALE = 0.25f * 1.4426950408889634f;
constexpr float SAFE2 = 3600.f;

constexpr unsigned WS_E = 0;
constexpr unsigned WS_FKV = 4096;
constexpr unsigned WS_FO = WS_FKV + 16384;
constexpr unsigned WS_FR = WS_FO + 8192;
constexpr unsigned WS_FY = WS_FR + 2048;
constexpr unsigned WS_K = 65536;
constexpr unsigned WS_V = WS_K + 2097152;
constexpr unsigned WS_O = WS_V + 2097152;
constexpr unsigned WS_Y = WS_O + 2097152;
constexpr unsigned WS_END = WS_Y + NSL * 2048;

constexpr int VT = 1056;
constexpr int WCH = 6400;
constexpr int L_X = 8 * WCH, L_G = L_X + 8 * 12288;
constexpr int L_V = 57344, L_Q = 116480, L_K7 = 131072, L_V7 = 139264;
constexpr int L_M = L_G + 8192;
constexpr int M_RDY = L_M, M_DONE = L_M + 32, M_QMAX = L_M + 48, M_KMAX = L_M + 52, M_CNT = L_M + 56, M_XRDY = L_M + 60;
constexpr int M_Z = L_M + 64, M_R = L_M + 192, M_ORD = L_M + 320, M_IDX = L_M + 352, M_CNT2 = L_M + 360, M_HP = L_M + 368;
constexpr int M_XSEL = L_M + 512, M_UPD = M_XSEL, M_RED = L_M + 1536, M_YB = L_M + 2048;
constexpr int L_O = 131072, L_BYTES = 163840;
static_assert(M_YB + 4096 == L_BYTES, "lds map");
__device__ __forceinline__ constexpr int kbase(int s) { return s < 7 ? s * 8192 : L_K7; }
__device__ __forceinline__ constexpr int vbase(int s) { return s < 7 ? L_V + s * 8 * VT : L_V7; }

#define MFMA32(a, b, c) __builtin_amdgcn_mfma_f32_32x32x16_bf16((a), (b), (c), 0, 0, 0)
#define WAITV(n) asm volatile("s_waitcnt vmcnt(" #n ")" ::: "memory")
#define BAR() asm volatile("s_waitcnt lgkmcnt(0)\n\ts_barrier" ::: "memory")
#define RFL(x) __builtin_amdgcn_readfirstlane(x)

__device__ __forceinline__ unsigned cvtpk(float lo, float hi) { const f32x2 v = {lo, hi}; return __builtin_bit_cast(unsigned, __builtin_convertvector(v, bf16x2)); }
__device__ __forceinline__ float bf_lo(unsigned u) { return __uint_as_float(u << 16); }
__device__ __forceinline__ float bf_hi(unsigned u) { return __uint_as_float(u & 0xffff0000u); }
__device__ __forceinline__ float rbf(float x) { return __uint_as_float(cvtpk(x, x) << 16); }
__device__ __forceinline__ float dot2(unsigned a, unsigned b, float c) { return __builtin_amdgcn_fdot2_f32_bf16(__builtin_bit_cast(bf16x2, a), __builtin_bit_cast(bf16x2, b), c, false); }
__device__ __forceinline__ float sigm(float z) { return __builtin_amdgcn_rcpf(1.f + __builtin_amdgcn_exp2f(-1.4426950408889634f * z)); }
template <int CTRL, int RM = 0xf> __device__ __forceinline__ unsigned dpp(unsigned v) { return __builtin_amdgcn_update_dpp(0u, v, CTRL, RM, 0xf, true); }
__device__ __forceinline__ unsigned wave_umax(unsigned v) {
    v = max(v, dpp<0x111>(v)); v = max(v, dpp<0x112>(v)); v = max(v, dpp<0x114>(v)); v = max(v, dpp<0x118>(v));
    v = max(v, dpp<0x142, 0xa>(v)); v = max(v, dpp<0x143, 0xc>(v));
    return __builtin_amdgcn_readlane(v, 63);
}

template <bool SC1> __device__ __forceinline__ void glds16(const void* sbase, unsigned voff, unsigned lds_base) {
    if constexpr (SC1) asm volatile("s_mov_b32 m0, %2\n\ts_nop 0\n\tglobal_load_lds_dwordx4 %0, %1 sc1" :: "v"(voff), "s"(sbase), "s"(lds_base) : "memory");
    else asm volatile("s_mov_b32 m0, %2\n\ts_nop 0\n\tglobal_load_lds_dwordx4 %0, %1" :: "v"(voff), "s"(sbase), "s"(lds_base) : "memory");
}
template <bool SC1> __device__ __forceinline__ void glds16x4(const void* sbase, unsigned voff, unsigned lds_base) {
    if constexpr (SC1) asm volatile("s_mov_b32 m0, %2\n\ts_nop 0\n\tglobal_load_lds_dwordx4 %0, %1 sc1\n\tglobal_load_lds_dwordx4 %0, %1 offset:1024 sc1\n\t"
                                    "global_load_lds_dwordx4 %0, %1 offset:2048 sc1\n\tglobal_load_lds_dwordx4 %0, %1 offset:3072 sc1" :: "v"(voff), "s"(sbase), "s"(lds_base) : "memory");
    else asm volatile("s_mov_b32 m0, %2\n\ts_nop 0\n\tglobal_load_lds_dwordx4 %0, %1\n\tglobal_load_lds_dwordx4 %0, %1 offset:1024\n\t"
                      "global_load_lds_dwordx4 %0, %1 offset:2048\n\tglobal_load_lds_dwordx4 %0, %1 offset:3072" :: "v"(voff), "s"(sbase), "s"(lds_base) : "memory");
}
__device__ __forceinline__ void glds16_m(const void* sbase, unsigned voff, unsigned lds_base, u64 mask) {
    asm volatile("s_mov_b32 m0, %2\n\ts_mov_b64 exec, %3\n\tglobal_load_lds_dwordx4 %0, %1\n\ts_mov_b64 exec, -1" :: "v"(voff), "s"(sbase), "s"(lds_base), "s"(mask) : "memory");
}
__device__ __forceinline__ bf16x8 lds_b128(const LAS char* p) { return *(const LAS bf16x8*)p; }
typedef short v4i16_t __attribute__((ext_vector_type(4)));
__device__ __forceinline__ s16x4 lds_tr(const LAS char* p) { return __builtin_bit_cast(s16x4, __builtin_amdgcn_ds_read_tr16_b64_v4i16((LAS v4i16_t*)p)); }
__device__ __forceinline__ bf16x8 join8(s16x4 a, s16x4 b) {
    typedef short s16x8 __attribute__((ext_vector_type(8)));
    s16x8 r = {a[0], a[1], a[2], a[3], b[0], b[1], b[2], b[3]};
    return __builtin_bit_cast(bf16x8, r);
}
__device__ __forceinline__ u64 ld_agent(const char* p) { return __hip_atomic_load((gu64*)p, __ATOMIC_RELAXED, __HIP_MEMORY_SCOPE_AGENT); }
__device__ __forceinline__ unsigned lds_ld(const LAS char* p) { return __hip_atomic_load((const LAS unsigned*)p, __ATOMIC_RELAXED, __HIP_MEMORY_SCOPE_WORKGROUP); }
__device__ __forceinline__ void lds_st(LAS char* p, unsigned v) { __hip_atomic_store((LAS unsigned*)p, v, __ATOMIC_RELAXED, __HIP_MEMORY_SCOPE_WORKGROUP); }
__device__ __forceinline__ void lds_max2_lane0(unsigned addr, unsigned a, unsigned b) {
    asm volatile("s_mov_b64 exec, 1\n\tds_max_u32 %0, %1\n\tds_max_u32 %0, %2 offset:4\n\ts_mov_b64 exec, -1" :: "v"(addr), "v"(a), "v"(b) : "memory");
}
__device__ __forceinline__ void lds_add_lane0(unsigned addr, unsigned a) { asm volatile("s_mov_b64 exec, 1\n\tds_add_u32 %0, %1\n\ts_mov_b64 exec, -1" :: "v"(addr), "v"(a) : "memory"); }
__device__ __forceinline__ void lds_st_lane0(unsigned addr, unsigned a) { asm volatile("s_mov_b64 exec, 1\n\tds_write_b32 %0, %1\n\ts_mov_b64 exec, -1" :: "v"(addr), "v"(a) : "memory"); }
__device__ __forceinline__ void st_agent_lane0(const void* sbase, unsigned voff, u64 v) {
    asm volatile("s_mov_b64 exec, 1\n\tglobal_store_dwordx2 %0, %1, %2 sc1\n\ts_mov_b64 exec, -1" :: "v"(voff), "v"(v), "s"(sbase) : "memory");
}
template <int SLEEP = 1> __device__ __forceinline__ unsigned lds_wait(const LAS char* p, unsigned mask, unsigned want) {
    unsigned v;
#pragma unroll 1
    for (int i = 0; i < (1 << 17); ++i) { v = RFL(lds_ld(p)); if ((v & mask) == want) break; __builtin_amdgcn_s_sleep(SLEEP); }
    asm volatile("" ::: "memory");
    return v;
}
constexpr unsigned RDYN = 4;
constexpr int SPIN = 1 << 14;

#define A_E(r) "v_exp_f32 v" #r ", v" #r "\n\t"
#define A_C(d, a, b) "v_cvt_pk_bf16_f32 v" #d ", v" #a ", v" #b "\n\t"
#define A_M(d, a, b, c) "v_mfma_f32_32x32x16_bf16 " d ", " a ", " b ", " c "\n\t"
#define A_R(d, ad) "ds_read_b128 " d ", " ad "\n\t"
#define A_SA "v[64:79]"
#define A_SB "v[80:95]"
#define A_OA "v[96:111]"
#define A_OB "v[112:127]"
#define A_QA "v[128:131]"
#define A_QB "v[132:135]"
#define A_P0 "v[136:139]"
#define A_P1 "v[140:143]"
#define A_K "v[144:147]"
#define A_VX "v[148:151]"
#define A_VY "v[152:155]"
#define A_V1 "v[156:159]"
#define A_TILE(VC, VN, AV1, AK, AV0, EXTRA)                                                                                          \
    A_E(64) A_E(65) A_M(A_OB, A_V1, A_P1, A_OB)   A_R(A_V1, AV1)                                              \
    A_E(66) A_E(67) A_C(136, 64, 65) A_E(68) A_E(69) A_C(137, 66, 67)                                                                \
    A_M(A_SB, A_K, A_QB, "0")   A_R(A_K, AK) A_R(VN, AV0) EXTRA                                                      \
    A_E(70) A_E(71) A_C(138, 68, 69) A_E(72) A_E(73) A_C(139, 70, 71) A_E(74) A_E(75)                                                \
    A_M(A_OA, VC, A_P0, A_OA)                                                                                     \
    A_C(140, 72, 73) A_E(76) A_E(77) A_C(141, 74, 75) A_E(78) A_E(79) A_C(142, 76, 77) A_C(143, 78, 79)                              \
    A_E(80) A_E(81) "s_waitcnt lgkmcnt(0)\n\t" A_M(A_OA, A_V1, A_P1, A_OA)                                        \
    A_E(82) A_E(83) A_C(136, 80, 81) A_E(84) A_E(85) A_C(137, 82, 83)                                                                \
    A_M(A_SA, A_K, A_QA, "0")                                                                                    \
    A_E(86) A_E(87) A_C(138, 84, 85) A_E(88) A_E(89) A_C(139, 86, 87) A_E(90) A_E(91)                                                \
    A_M(A_OB, VC, A_P0, A_OB)                                                                                     \
    A_C(140, 88, 89) A_E(92) A_E(93) A_C(141, 90, 91) A_E(94) A_E(95) A_C(142, 92, 93) A_C(143, 94, 95)
#define A_KA(s, t) "v160 offset:" #s "*8192+" #t "*1024"
#define A_V0A(s, t) "v161 offset:(" #s "*8+" #t ")*1056"
#define A_V1A(s, t) "v162 offset:(" #s "*8+" #t ")*1056"
#define A_K7(t) "v163 offset:" #t "*1024"
#define A_V07(t) "v164 offset:" #t "*1056"
#define A_V17(t) "v165 offset:" #t "*1056"
#define A_SLOT(s, s1, NK, NV0, AFTER)                                                                                                \
    A_TILE(A_VX, A_VY, A_V1A(s, 0), A_KA(s, 1), A_V0A(s, 1), "ds_read_b32 v166, v167 offset:" #s1 "*4\n\t")                          \
    A_TILE(A_VY, A_VX, A_V1A(s, 1), A_KA(s, 2), A_V0A(s, 2), "") A_TILE(A_VX, A_VY, A_V1A(s, 2), A_KA(s, 3), A_V0A(s, 3), "")        \
    A_TILE(A_VY, A_VX, A_V1A(s, 3), A_KA(s, 4), A_V0A(s, 4), "") A_TILE(A_VX, A_VY, A_V1A(s, 4), A_KA(s, 5), A_V0A(s, 5), "")        \
    A_TILE(A_VY, A_VX, A_V1A(s, 5), A_KA(s, 6), A_V0A(s, 6), "") A_TILE(A_VX, A_VY, A_V1A(s, 6), A_KA(s, 7), A_V0A(s, 7), "")        \
    "v_readfirstlane_b32 %[t0], v166\n\ts_cmp_eq_u32 %[t0], 4\n\ts_cbranch_scc0 H" #s "_%=\n\tC" #s "_%=:\n\t"                         \
    A_TILE(A_VY, A_VX, A_V1A(s, 7), NK, NV0, "") AFTER
#define A_DONE(v) "v_mov_b32 v172, " #v "\n\tds_write_b32 v171, v172\n\t"
#define A_WAIT(s, s1)                                                                                                                \
    "H" #s "_%=:\n\ts_mov_b32 %[t1], 131072\n\tW" #s "_%=:\n\ts_and_b32 %[slot], %[t0], 0xff\n\ts_cmp_eq_u32 %[slot], 4\n\t"            \
    "s_cbranch_scc1 R" #s "_%=\n\ts_sub_u32 %[t1], %[t1], 1\n\ts_cbranch_scc1 C" #s "_%=\n\ts_sleep 1\n\t"                             \
    "ds_read_b32 v166, v167 offset:" #s1 "*4\n\ts_waitcnt lgkmcnt(0)\n\tv_readfirstlane_b32 %[t0], v166\n\ts_nop 0\n\ts_branch W" #s "_%=\n\t" \
    "R" #s "_%=:\n\ts_cmp_eq_u32 %[t0], 4\n\ts_cbranch_scc1 C" #s "_%=\n\t"                                                            \
    "v_add_u32 v172, (" #s "*8+7)*1056, v162\n\ts_mov_b32 %[slot], " #s1 "\n\ts_branch U_%=\n\t"
#define ATTN_LOOP                                                                                                                    \
    A_R(A_K, A_KA(0, 0)) A_R(A_VX, A_V0A(0, 0)) A_R(A_V1, A_V1A(0, 0))                                                               \
    "v_mov_b32 v140, 0\n\tv_mov_b32 v141, 0\n\tv_mov_b32 v142, 0\n\tv_mov_b32 v143, 0\n\ts_waitcnt lgkmcnt(0)\n\t"                     \
    A_M(A_SA, A_K, A_QA, "0") "s_nop 7\n\ts_nop 3\n\t"                                                                                \
    A_SLOT(0, 1, A_KA(1, 0), A_V0A(1, 0), "") A_SLOT(1, 2, A_KA(2, 0), A_V0A(2, 0), "") A_SLOT(2, 3, A_KA(3, 0), A_V0A(3, 0), "")    \
    A_SLOT(3, 4, A_KA(4, 0), A_V0A(4, 0), A_DONE(1)) A_SLOT(4, 5, A_KA(5, 0), A_V0A(5, 0), "") A_SLOT(5, 6, A_KA(6, 0), A_V0A(6, 0), "") \
    A_SLOT(6, 7, A_K7(0), A_V07(0), A_DONE(2))                                                                                       \
    A_TILE(A_VX, A_VY, A_V17(0), A_K7(1), A_V07(1), "") A_TILE(A_VY, A_VX, A_V17(1), A_K7(2), A_V07(2), "")                          \
    A_TILE(A_VX, A_VY, A_V17(2), A_K7(3), A_V07(3), "") A_TILE(A_VY, A_VX, A_V17(3), A_K7(4), A_V07(4), "")                          \
    A_TILE(A_VX, A_VY, A_V17(4), A_K7(5), A_V07(5), "") A_TILE(A_VY, A_VX, A_V17(5), A_K7(6), A_V07(6), "")                          \
    A_TILE(A_VX, A_VY, A_V17(6), A_K7(7), A_V07(7), "") A_TILE(A_VY, A_VX, A_V17(7), A_K7(7), A_V07(7), "")                          \
    "s_mov_b32 %[slot], 8\n\t"                                                                                                       \
    "X_%=:\n\ts_nop 1\n\t" A_M(A_OB, A_V1, A_P1, A_OB)   "s_waitcnt lgkmcnt(0)\n\ts_nop 7\n\ts_nop 7\n\ts_branch E_%=\n\t"   \
    A_WAIT(0, 1) A_WAIT(1, 2) A_WAIT(2, 3) A_WAIT(3, 4) A_WAIT(4, 5) A_WAIT(5, 6) A_WAIT(6, 7)                                       \
    "U_%=:\n\t" A_TILE(A_VY, A_VX, "v172", "v160", "v161", "") "s_branch X_%=\n\t"                                                    \
    "E_%=:\n\t"
#define ATTN_CLOB                                                                                                                    \
    "v64", "v65", "v66", "v67", "v68", "v69", "v70", "v71", "v72", "v73", "v74", "v75", "v76", "v77", "v78", "v79",                  \
    "v80", "v81", "v82", "v83", "v84", "v85", "v86", "v87", "v88", "v89", "v90", "v91", "v92", "v93", "v94", "v95",                  \
    "v136", "v137", "v138", "v139", "v140", "v141", "v142", "v143", "v144", "v145", "v146", "v147", "v148", "v149", "v150", "v151",  \
    "v152", "v153", "v154", "v155", "v156", "v157", "v158", "v159", "v166", "v172"

__device__ __forceinline__ int slot_block(int tb, int s) { const int k = s - 5; return s < 5 ? 4 * ((tb >> 2) ^ 1) + s - 1 : (tb & 4) + k + (k >= (tb & 3) ? 1 : 0); }

__device__ __forceinline__ void attn_slow_tile(const LAS char* kp, const LAS char* vp0, const LAS char* vp1, const bf16x8 q, f32x16& o, float& m) {
    const f32x16 z = {};
    f32x16 s = MFMA32(lds_b128(kp), q, z);
    float mx = s[0];
#pragma unroll
    for (int r = 1; r < 16; ++r) mx = fmaxf(mx, s[r]);
    auto sw = __builtin_amdgcn_permlane32_swap(__float_as_uint(mx), __float_as_uint(mx), false, false);
    mx = fmaxf(__uint_as_float(sw[0]), __uint_as_float(sw[1]));
    const float mn = fmaxf(m, mx), al = __builtin_amdgcn_exp2f(m - mn);
    m = mn;
    u32x4 p0, p1;
#pragma unroll
    for (int j = 0; j < 4; ++j) {
        p0[j] = cvtpk(__builtin_amdgcn_exp2f(s[2 * j] - mn), __builtin_amdgcn_exp2f(s[2 * j + 1] - mn));
        p1[j] = cvtpk(__builtin_amdgcn_exp2f(s[8 + 2 * j] - mn), __builtin_amdgcn_exp2f(s[8 + 2 * j + 1] - mn));
    }
#pragma unroll
    for (int r = 0; r < 16; ++r) o[r] *= al;
    o = MFMA32(lds_b128(vp0), __builtin_bit_cast(bf16x8, p0), o);
    o = MFMA32(lds_b128(vp1), __builtin_bit_cast(bf16x8, p1), o);
}

typedef unsigned u32x16 __attribute__((ext_vector_type(16)));
typedef const unsigned short* cptr16;
__global__ __launch_bounds__(512, 2) void fused_kernel(cptr16 x_, cptr16 wqkv_, cptr16 wo_, cptr16 wr_, cptr16 wg_, cptr16 wu_, cptr16 wd_, cptr16 gw_, char* ws_, unsigned short* out_) {
    u32x16 ka; u32x4 kb;
    asm volatile("s_load_dwordx16 %0, %2, 0x0\n\ts_load_dwordx4 %1, %2, 0x40" : "=&s"(ka), "=&s"(kb) : "s"(__builtin_amdgcn_kernarg_segment_ptr()) : "memory");
    extern __shared__ __attribute__((aligned(16))) char lds_[];
    LAS char* lds = (LAS char*)lds_;
    const unsigned lds0 = (unsigned)(uintptr_t)lds;
    const int tid = threadIdx.x, lane = tid & 63, wid = RFL(tid >> 6);
    const int bid = blockIdx.x, xcd_ = bid & 7, u_ = bid >> 3;
    const int h = 8 * (xcd_ & 3) + (u_ & 7), tb = 4 * (xcd_ >> 2) + (u_ >> 3), c0 = ((h & 7) + 2 * (tb & 3)) & 7;
    const int r32 = lane & 31, hh = lane >> 5, i16 = lane & 15, g1 = (lane >> 4) & 1, q4 = i16 >> 2, p4 = i16 & 3;
    unsigned pf = 0;
    { u64 pc; unsigned len;
      asm volatile("s_getpc_b64 %0\n.Lfk_start:\n\ts_mov_b32 %1, .Lfk_end-.Lfk_start" : "=s"(pc), "=s"(len));
      const unsigned off = (unsigned)(((bid >> 3) * 12 + lane) * 128);
      if (wid == 0 && lane < 12 && off < len) asm volatile("global_load_dword %0, %1, %2" : "+v"(pf) : "v"(off), "s"(pc) : "memory"); }

    const int seg = 25 * wid + (lane >> 1), wm = seg < 68 ? 0 : seg < 132 ? 1 : 2;
    unsigned woff = (unsigned)(((seg - (wm == 0 ? 0 : wm == 1 ? 68 : 132)) & 63) * 3072 + wm * 1024 + h * 32 + (lane & 1) * 16);
    const int tok0 = tb * 256 + wid * 32;
    unsigned xoff[4];
#pragma unroll
    for (int q = 0; q < 4; ++q) xoff[q] = (unsigned)((tok0 + 8 * q + (lane >> 3)) * 1024 + (((lane & 7) ^ ((4 * q + (lane >> 4)) & 7)) * 16));
    asm volatile("" : "+v"(woff), "+v"(xoff[0]), "+v"(xoff[1]), "+v"(xoff[2]), "+v"(xoff[3]));
    asm volatile("s_waitcnt lgkmcnt(0)" : "+s"(ka), "+s"(kb) :: "memory");
#define KARG(T, v, i) ((T)(GAS void*)(((u64)(v)[(i) + 1] << 32) | (v)[i]))
    const unsigned short* const x = KARG(const unsigned short*, ka, 0); const unsigned short* const wqkv = KARG(const unsigned short*, ka, 2);
    const unsigned short* const wo = KARG(const unsigned short*, ka, 4); const unsigned short* const wr = KARG(const unsigned short*, ka, 6);
    const unsigned short* const wg = KARG(const unsigned short*, ka, 8); const unsigned short* const wu = KARG(const unsigned short*, ka, 10);
    const unsigned short* const wd = KARG(const unsigned short*, ka, 12); const unsigned short* const gw = KARG(const unsigned short*, ka, 14);
    char* const ws = KARG(char*, kb, 0); unsigned short* const out = KARG(unsigned short*, kb, 2);
    unsigned gwv, wrv;
    const unsigned xring = lds0 + L_X + wid * 3 * 4096, wdst = lds0 + wid * 800;
    const u64 wmask = wid == 2 ? 0x3fff00fffffffull : wid == 7 ? 0x3ffffffffffull : 0x3ffffffffffffull;
#define CH(i) ((c0 + (i)) & 7)
#define WLD(i) glds16_m((const char*)wqkv + CH(i) * 196608, woff, RFL(wdst + (i) * WCH), wmask)
#define XLD(i, q) glds16<false>((const char*)x + CH(i) * 128, xoff[q], RFL(xring + ((i) % 3) * 4096 + (q) * 1024))
#define GLOAD(i) do { WLD(i); XLD(i, 0); XLD(i, 1); XLD(i, 2); XLD(i, 3); } while (0)
    GLOAD(0);
    asm volatile("global_load_ushort %0, %1, %2" : "=v"(gwv) : "v"(tid * 2), "s"(gw) : "memory");
    asm volatile("global_load_ushort %0, %1, %2" : "=v"(wrv) : "v"(tid * 2), "s"(wr) : "memory");
    u32x2 ep; asm volatile("global_load_dwordx2 %0, %1, %2 sc1" : "=v"(ep) : "v"(WS_E), "s"(ws) : "memory");
    asm volatile("s_barrier" ::: "memory");
    GLOAD(1); GLOAD(2);
    if (tid < 28) lds_st(lds + (tid < 16 ? L_M + tid * 4 : M_ORD + (tid - 16) * 4), 0u);
    if (tid == 28 || tid == 29) *(LAS u32x2*)(lds + L_G + (tid == 28 ? 64 : 192)) = (u32x2){0u, 0u};
    asm volatile("s_waitcnt vmcnt(10)" : "+v"(gwv), "+v"(wrv), "+v"(pf), "+v"(ep) :: "memory");
    *(LAS u32x2*)(lds + L_G + (((tid >> 4) - 4 * CH(0)) & 31) * 256 + ((tid >> 2) & 1) * 128 + ((tid >> 3) & 1) * 32 + (tid & 3) * 8) = (u32x2){gwv | (wrv << 16), 0u};
    const u64 T = (((u64)RFL(ep[1]) << 32) | RFL(ep[0])) + 1;
    asm volatile("s_waitcnt lgkmcnt(0)\n\ts_barrier" :: "s"(T) : "memory");
    const LAS char* wa = lds + (g1 ? 2176 : 0) + (8 * hh + q4) * 32 + p4 * 8;
    const LAS char* wb = g1 ? (p4 == 0 ? lds + L_G + hh * 32 + q4 * 8 : lds + L_G + 64) : lds + 4224 + (8 * hh + q4) * 32 + p4 * 8;
    const int wbincA = g1 ? (p4 == 0 ? 256 : 0) : 512, wbincB = g1 ? (p4 == 0 ? 256 : 0) : WCH - 1536;
    const LAS char* xr = lds + L_X + wid * 3 * 4096 + r32 * 128;
    const int swz = (r32 >> 1) & 7;
    f32x16 acc1 = {}, acc2 = {};
#pragma unroll
    for (int c = 0; c < 8; ++c) {
        if (c >= 1) { if (c <= 5) WAITV(10); else if (c == 6) WAITV(5); else WAITV(0); BAR(); }
        bf16x8 xf[4], fa[4], fb[4];
#pragma unroll
        for (int i = 0; i < 4; ++i) xf[i] = lds_b128(xr + (c % 3) * 4096 + (((2 * i + hh) ^ swz) * 16));
#define WFR(i) do { fa[i] = join8(lds_tr(wa + c * WCH + (i) * 512), lds_tr(wa + c * WCH + (i) * 512 + 128)); fb[i] = join8(lds_tr(wb), lds_tr(wb + 128)); wb += (i) == 3 ? wbincB : wbincA; } while (0)
#define KST(i) do { acc1 = MFMA32(fa[i], xf[i], acc1); acc2 = MFMA32(xf[i], fb[i], acc2); } while (0)
        WFR(0); WFR(1);
        asm volatile("" : "+v"(xf[0]), "+v"(xf[1]), "+v"(xf[2]), "+v"(xf[3]));
        KST(0); __builtin_amdgcn_sched_barrier(0);
        if (c + 3 < 8) { WLD(c + 3); XLD(c + 3, 0); }
        WFR(2); KST(1); __builtin_amdgcn_sched_barrier(0);
        if (c + 3 < 8) XLD(c + 3, 1);
        WFR(3); KST(2); __builtin_amdgcn_sched_barrier(0);
        if (c + 3 < 8) XLD(c + 3, 2);
        KST(3); __builtin_amdgcn_sched_barrier(0);
        if (c + 3 < 8) XLD(c + 3, 3);
    }
#undef WFR
#undef KST
#undef WLD
#undef XLD
#undef GLOAD
    u32x4 qf, kf, vf;
#pragma unroll
    for (int j = 0; j < 4; ++j) {
        kf[j] = cvtpk(acc1[8 + 2 * j], acc1[8 + 2 * j + 1]);
        auto sw = __builtin_amdgcn_permlane16_swap(cvtpk(acc2[2 * j], acc2[2 * j + 1]), cvtpk(acc2[8 + 2 * j], acc2[8 + 2 * j + 1]), false, false);
        vf[j] = sw[0];
    }
    const __amdgpu_buffer_rsrc_t rs = __builtin_amdgcn_make_buffer_rsrc(ws, 0, WS_END, 0x00020000);
    const int tile = tb * 8 + wid;
    const int vpos = g1 * 512 + hh * 256 + i16 * 16;
    __builtin_amdgcn_raw_buffer_store_b128(kf, rs, lane * 16, WS_K + (h * 64 + tile) * 1024, 16);
    __builtin_amdgcn_raw_buffer_store_b128(vf, rs, vpos, WS_V + (h * 64 + tile) * 1024, 16);
    float qn = 0.f, kn = 0.f;
#pragma unroll
    for (int j = 0; j < 4; ++j) { qf[j] = cvtpk(acc1[2 * j] * QSCALE, acc1[2 * j + 1] * QSCALE); qn = dot2(qf[j], qf[j], qn); kn = dot2(kf[j], kf[j], kn); }
    BAR();
    *(LAS u32x4*)(lds + L_Q + wid * 1024 + lane * 16) = qf;
    *(LAS u32x4*)(lds + kbase(0) + wid * 1024 + lane * 16) = kf;
    *(LAS u32x4*)(lds + vbase(0) + wid * VT + vpos) = vf;
    if (tid < 64) { LAS char* p = lds + vbase(tid >> 3) + (tid & 7) * VT;
        *(LAS u32x4*)(p + 1024) = (u32x4){0x3f803f80u, 0x3f803f80u, 0x3f803f80u, 0x3f803f80u}; *(LAS u32x4*)(p + 1040) = (u32x4){0u, 0u, 0u, 0u}; }
    if (wid == (h >> 2) && (r32 >> 1) == 8) {
#pragma unroll
        for (int q = 0; q < 4; ++q) *(LAS f32x4*)(lds + M_Z + (r32 & 1) * 128 + (8 * q + 4 * hh) * 4) = (f32x4){acc2[4 * q], acc2[4 * q + 1], acc2[4 * q + 2], acc2[4 * q + 3]};
    }
    const unsigned qmx = wave_umax(__float_as_uint(qn * 2.02f)), kmx = wave_umax(__float_as_uint(kn * 2.02f));
    const u64 kvflag = (T << 16) | ((kmx + 0xffffu) >> 16);
    lds_max2_lane0(lds0 + M_QMAX, qmx, kmx);
    WAITV(0);
    st_agent_lane0(ws, WS_FKV + (h * 64 + tile) * 8, kvflag);
    BAR();
    const float qmax2 = __uint_as_float(RFL(lds_ld(lds + M_QMAX)));
    const int tt = tb * 8 + (h >> 2), cb = h & 3;
    const int nt = wid & 3, kh = wid >> 2;
    const int ncol = cb * 128 + nt * 32;
    float gate; u32x2 resid[2]; f32x4 upd[2]; int idx;

    if (wid < 4) {
        const u32x4 qa = *(const LAS u32x4*)(lds + L_Q + (2 * wid) * 1024 + lane * 16), qb = *(const LAS u32x4*)(lds + L_Q + (2 * wid + 1) * 1024 + lane * 16);
        const float kmax0 = __uint_as_float(RFL(lds_ld(lds + M_KMAX)));
        const bool ones = (r32 == 16 || r32 == 28), zer = r32 >= 16 && !ones;
        const unsigned v0lane = ones ? 1024 : zer ? 1040 : hh * 256 + r32 * 16, v1lane = ones ? 1024 : zer ? 1040 : 512 + hh * 256 + r32 * 16;
        f32x16 oa = {}, ob = {};
        int slot = 0;
        __builtin_amdgcn_s_setprio(1);
        if (qmax2 * kmax0 <= SAFE2) {
            int t0, t1;
            asm volatile(
                ATTN_LOOP
                : "+{v[96:111]}"(oa), "+{v[112:127]}"(ob), [slot] "=&s"(slot), [t0] "=&s"(t0), [t1] "=&s"(t1)
                : "{v[128:131]}"(qa), "{v[132:135]}"(qb), "{v160}"(lds0 + lane * 16), "{v161}"(lds0 + L_V + v0lane), "{v162}"(lds0 + L_V + v1lane),
                  "{v163}"(lds0 + L_K7 + lane * 16), "{v164}"(lds0 + L_V7 + v0lane), "{v165}"(lds0 + L_V7 + v1lane), "{v167}"(lds0 + M_RDY),
                  "{v171}"(lds0 + M_DONE + wid * 4)
                : "memory", "scc", ATTN_CLOB);
        }
        if (slot < 8) {
            float ma = slot == 0 ? -3.0e38f : 0.f, mb = ma;
            for (int s = slot; s < 8; ++s) {
                if (s > 0) lds_wait(lds + M_RDY + s * 4, 0xffu, RDYN);
                if (s >= 4) lds_st(lds + M_DONE + wid * 4, s == 7 ? 2u : 1u);
                const int kb = s < 7 ? s * 8192 : L_K7, vb = s < 7 ? L_V + s * 8 * VT : L_V7;
                for (int t = 0; t < 8; ++t) {
                    attn_slow_tile(lds + kb + t * 1024 + lane * 16, lds + vb + t * VT + v0lane, lds + vb + t * VT + v1lane, __builtin_bit_cast(bf16x8, qa), oa, ma);
                    attn_slow_tile(lds + kb + t * 1024 + lane * 16, lds + vb + t * VT + v0lane, lds + vb + t * VT + v1lane, __builtin_bit_cast(bf16x8, qb), ob, mb);
                }
            }
        }
        __builtin_amdgcn_s_setprio(0);
        const float ia = __builtin_amdgcn_rcpf(hh ? oa[12] : oa[8]), ib = __builtin_amdgcn_rcpf(hh ? ob[12] : ob[8]);
        u32x4 fa, fb;
#pragma unroll
        for (int j = 0; j < 4; ++j) { fa[j] = cvtpk(oa[2 * j] * ia, oa[2 * j + 1] * ia); fb[j] = cvtpk(ob[2 * j] * ib, ob[2 * j + 1] * ib); }
        __builtin_amdgcn_raw_buffer_store_b128(fa, rs, WS_O + ((tb * 8 + 2 * wid) * 32 + h) * 1024 + lane * 16, 0, 16);
        __builtin_amdgcn_raw_buffer_store_b128(fb, rs, WS_O + ((tb * 8 + 2 * wid + 1) * 32 + h) * 1024 + lane * 16, 0, 16);
        WAITV(0);
        st_agent_lane0(ws, WS_FO + ((tb * 4 + wid) * 32 + h) * 8, T);
        lds_st_lane0(lds0 + M_DONE + wid * 4, 3u);
#pragma unroll
        for (int q = 0; q < 2; ++q) resid[q] = *(const u32x2*)(x + (size_t)(tt * 32 + r32) * DM + ncol + 8 * q + 4 * hh);
        gate = rbf(sigm(rbf(*(const LAS float*)(lds + M_Z + r32 * 4))));
        lds_wait<1>(lds + M_XRDY, 1u, 1u);
        idx = 2047 - (int)(RFL(lds_ld(lds + M_IDX)) & 2047u);
        upd[0] = upd[1] = (f32x4){0.f, 0.f, 0.f, 0.f};
        if ((idx >> 5) == tt) {
            BAR();
#pragma unroll
            for (int q = 0; q < 2; ++q) upd[q] = *(const LAS f32x4*)(lds + M_UPD + (nt * 32 + 8 * q + 4 * hh) * 4);
        }
        BAR();
    } else {
        const int sw_ = wid - 4, st = tid - 256;
        __builtin_amdgcn_s_setprio(3);
        if (sw_ == 0) {
            const int fs = lane >> 3;
            const char* fp = ws + WS_FKV + (h * 64 + (fs ? slot_block(tb, fs) : tb) * 8 + (lane & 7)) * 8;
            int nslot = 1;
            const unsigned ehi = (unsigned)(T >> 16), elo = (unsigned)T << 16;
            __builtin_amdgcn_s_sleep(8);
            { const char* fp1 = ws + WS_FKV + (h * 64 + slot_block(tb, 1) * 8 + (lane & 7)) * 8;
              for (int it = 0; it < SPIN; ++it) { const u64 f = ld_agent(fp1); if (__all((unsigned)(f >> 32) == ehi && ((unsigned)f & 0xffff0000u) == elo)) {
                        unsigned km = (unsigned)f << 16;
                        km = max(km, dpp<0x111>(km)); km = max(km, dpp<0x112>(km)); km = max(km, dpp<0x114>(km));
                        const u64 bad = __ballot(!(qmax2 * __uint_as_float(km) <= SAFE2));
                        lds_st_lane0(lds0 + M_ORD + 4, 0x1001u | (((bad >> 7) & 1) ? 0x100u : 0u)); nslot = 2; break; } } }
            if (nslot == 2) glds16x4<true>(ws + WS_K + h * 65536 + slot_block(tb, 1) * 8192, lane * 16, RFL(lds0 + 8192));
            WAITV(0);
            lds_add_lane0(lds0 + M_RDY + 4, 1u);
            for (int it = 0; it < SPIN && nslot < 8; ++it) {
                const u64 f = ld_agent(fp);
                const unsigned flo = (unsigned)f, fhi = (unsigned)(f >> 32);
                const u64 okm = __ballot(fhi == ehi && (flo & 0xffff0000u) == elo);
                if (((okm >> (nslot * 8)) & 0xff) == 0xff) {
                    unsigned km = flo << 16;
                    km = max(km, dpp<0x111>(km)); km = max(km, dpp<0x112>(km)); km = max(km, dpp<0x114>(km));
                    const u64 bad = __ballot(!(qmax2 * __uint_as_float(km) <= SAFE2));
                    do {
                        lds_st_lane0(lds0 + M_ORD + nslot * 4, 0x1000u | (unsigned)nslot | (((bad >> (nslot * 8 + 7)) & 1) ? 0x100u : 0u));
                        ++nslot;
                    } while (nslot < 8 && ((okm >> (nslot * 8)) & 0xff) == 0xff);
                }
            }
            for (; nslot < 8; ++nslot) lds_st_lane0(lds0 + M_ORD + nslot * 4, 0x1000u | (unsigned)nslot);
        } else if (sw_ < 3) {
            const char* src = ws + (sw_ == 1 ? WS_K : WS_V) + h * 65536;
            int pend = 0; unsigned pb = 0;
            for (int s = 1; s < 8; ++s) {
                unsigned v = RFL(lds_ld(lds + M_ORD + s * 4));
                if (!(v & 0x1000u)) {
                    if (pend) { WAITV(0); lds_add_lane0(lds0 + M_RDY + pend * 4, pb); pend = 0; }
                    v = lds_wait<2>(lds + M_ORD + s * 4, 0x1000u, 0x1000u);
                }
                const int blk = slot_block(tb, s);
                const int base = sw_ == 1 ? (s < 7 ? s * 8192 : L_K7) : (s < 7 ? L_V + s * 8 * VT : L_V7);
                if (sw_ == 1) { if (s > 1) glds16x4<true>(src + blk * 8192, lane * 16, RFL(lds0 + base)); glds16x4<true>(src + blk * 8192 + 4096, lane * 16, RFL(lds0 + base + 4096)); }
                else {
#pragma unroll
                    for (int t = 0; t < 4; ++t) glds16<true>(src + blk * 8192 + t * 1024, lane * 16, RFL(lds0 + base + t * VT));
                    if (s > 1) {
#pragma unroll
                        for (int t = 4; t < 8; ++t) glds16<true>(src + blk * 8192 + t * 1024, lane * 16, RFL(lds0 + base + t * VT)); }
                }
                if (pend) { WAITV(8); lds_add_lane0(lds0 + M_RDY + pend * 4, pb); }
                pend = s; pb = (s == 1 ? 1u : 2u) | (sw_ == 1 ? (v & 0x100u) : 0u);
            }
            WAITV(0);
            lds_add_lane0(lds0 + M_RDY + pend * 4, pb);
        } else if (cb == 0) {
            const unsigned u = cvtpk(*(const LAS float*)(lds + M_R + r32 * 4), 0.f) & 0xffffu;
            const unsigned key = (((u & 0x8000u) ? (~u & 0xffffu) : (u | 0x8000u)) << 11) | (2047u - (unsigned)(tt * 32 + r32));
            const unsigned best = wave_umax(key);
            st_agent_lane0(ws, WS_FR + tt * 8, (T << 27) | best);
        }
        if (sw_ == 3) {
            lds_wait<2>(lds + M_ORD + 4, 0x1000u, 0x1000u);
#pragma unroll
            for (int t = 4; t < 8; ++t) glds16<true>(ws + WS_V + h * 65536 + slot_block(tb, 1) * 8192 + t * 1024, lane * 16, RFL(lds0 + L_V + 8 * VT + t * VT));
            WAITV(0);
            lds_add_lane0(lds0 + M_RDY + 4, 1u);
            lds_wait<4>(lds + M_ORD + 7 * 4, 0x1000u, 0x1000u);
        }
        __builtin_amdgcn_s_setprio(2);
        const int xa = bid & 7, ub = bid >> 3, sa = (ub >> 3) * 8 + xa, sl = sa * 8 + (ub & 7);
        const bool has_slice = ub < 24 && sl < NSL;
        unsigned wgv[8], wuv[8], wdv[8];
        if (has_slice) {
            const __amdgpu_buffer_rsrc_t rg_ = __builtin_amdgcn_make_buffer_rsrc((void*)wg, 0, DM * FF * 2, 0x00020000);
            const __amdgpu_buffer_rsrc_t ru_ = __builtin_amdgcn_make_buffer_rsrc((void*)wu, 0, DM * FF * 2, 0x00020000);
            const __amdgpu_buffer_rsrc_t rd_ = __builtin_amdgcn_make_buffer_rsrc((void*)wd, 0, DM * FF * 2, 0x00020000);
            const int vo = (st >> 2) * 8 * FF * 2 + (st & 3) * 4;
#pragma unroll
            for (int i = 0; i < 8; ++i) { wgv[i] = __builtin_amdgcn_raw_buffer_load_b32(rg_, vo, sl * 16 + i * FF * 2, 0); wuv[i] = __builtin_amdgcn_raw_buffer_load_b32(ru_, vo, sl * 16 + i * FF * 2, 0); }
#pragma unroll
            for (int f = 0; f < 8; ++f) wdv[f] = __builtin_amdgcn_raw_buffer_load_b32(rd_, st * 4, (sl * 8 + f) * DM * 2, 0);
        }
        if (sw_ == 0) {
            unsigned best = 0;
            for (int it = 0; it < SPIN; ++it) {
                const u64 g = ld_agent(ws + WS_FR + lane * 8);
                best = (unsigned)g & 0x7ffffffu;
                if (__all((g >> 27) == (T & 0x1fffffffffull))) break;
            }
            best = wave_umax(best);
            const int id = 2047 - (int)(best & 2047u);
            const u32x4 xs = __builtin_amdgcn_raw_buffer_load_b128(__builtin_amdgcn_make_buffer_rsrc((void*)x, 0, 2048 * DM * 2, 0x00020000), lane * 16, id * DM * 2, 0);
            *(LAS u32x4*)(lds + M_XSEL + lane * 16) = xs;
            lds_st_lane0(lds0 + M_IDX, best);
            asm volatile("s_waitcnt lgkmcnt(0)" ::: "memory");
            lds_st_lane0(lds0 + M_XRDY, 1u);
        }
        lds_wait<6>(lds + M_XRDY, 1u, 1u);
        const unsigned rkey = RFL(lds_ld(lds + M_IDX));
        idx = 2047 - (int)(rkey & 2047u);
        if (has_slice) {
            const int rg8 = st >> 2;
            const u32x4 xs = *(const LAS u32x4*)(lds + M_XSEL + rg8 * 16);
            float g0 = 0.f, g1_ = 0.f, u0 = 0.f, u1 = 0.f;
#pragma unroll
            for (int p = 0; p < 4; ++p) {
                g0 = dot2(__builtin_amdgcn_perm(wgv[2 * p + 1], wgv[2 * p], 0x05040100u), xs[p], g0); g1_ = dot2(__builtin_amdgcn_perm(wgv[2 * p + 1], wgv[2 * p], 0x07060302u), xs[p], g1_);
                u0 = dot2(__builtin_amdgcn_perm(wuv[2 * p + 1], wuv[2 * p], 0x05040100u), xs[p], u0); u1 = dot2(__builtin_amdgcn_perm(wuv[2 * p + 1], wuv[2 * p], 0x07060302u), xs[p], u1);
            }
            auto a0 = __builtin_amdgcn_permlane32_swap(__float_as_uint(g0), __float_as_uint(u0), false, false);
            auto a1 = __builtin_amdgcn_permlane32_swap(__float_as_uint(g1_), __float_as_uint(u1), false, false);
            const float s0 = __uint_as_float(a0[0]) + __uint_as_float(a0[1]), s1 = __uint_as_float(a1[0]) + __uint_as_float(a1[1]);
            auto a2 = __builtin_amdgcn_permlane16_swap(__float_as_uint(s0), __float_as_uint(s1), false, false);
            float t = __uint_as_float(a2[0]) + __uint_as_float(a2[1]);
            t += __uint_as_float(dpp<0x128>(__float_as_uint(t))); t += __uint_as_float(dpp<0x124>(__float_as_uint(t)));
            if (((lane >> 2) & 3) == 0) *(LAS float*)(lds + M_RED + (sw_ * 16 + (lane >> 5) * 8 + (lane & 3) * 2 + g1) * 4) = t;
            asm volatile("s_waitcnt lgkmcnt(0)" ::: "memory");
            lds_add_lane0(lds0 + M_CNT, 1u);
            if (sw_ == 3) {
                lds_wait<1>(lds + M_CNT, 0xffu, 4u);
                f32x2 gu = {0.f, 0.f};
#pragma unroll
                for (int w = 0; w < 4; ++w) gu += (f32x2){*(const LAS float*)(lds + M_RED + (w * 16 + (lane & 7)) * 4), *(const LAS float*)(lds + M_RED + (w * 16 + 8 + (lane & 7)) * 4)};
                const unsigned r16 = cvtpk(gu[0], gu[1]);
                const float gs = bf_lo(r16), us = bf_hi(r16);
                const unsigned hb = cvtpk(rbf(gs * sigm(gs)) * us, 0.f);
                if (lane < 8) *(LAS unsigned short*)(lds + M_HP + lane * 2) = (unsigned short)hb;
                asm volatile("s_waitcnt lgkmcnt(0)" ::: "memory");
                lds_add_lane0(lds0 + M_CNT, 0x100u);
            }
            lds_wait<2>(lds + M_CNT, 0x100u, 0x100u);
            const u32x4 hp = *(const LAS u32x4*)(lds + M_HP);
            float y0 = 0.f, y1 = 0.f;
#pragma unroll
            for (int p = 0; p < 4; ++p) { y0 = dot2(__builtin_amdgcn_perm(wdv[2 * p + 1], wdv[2 * p], 0x05040100u), hp[p], y0); y1 = dot2(__builtin_amdgcn_perm(wdv[2 * p + 1], wdv[2 * p], 0x07060302u), hp[p], y1); }
            __builtin_amdgcn_raw_buffer_store_b64((u32x2){__float_as_uint(y0), __float_as_uint(y1)}, rs, st * 8, WS_Y + sl * 2048, 16);
            WAITV(0);
            st_agent_lane0(ws, WS_FY + (sw_ * 192 + sl) * 8, T);
        }
        __builtin_amdgcn_s_setprio(0);
#pragma unroll
        for (int q = 0; q < 2; ++q) resid[q] = *(const u32x2*)(x + (size_t)(tt * 32 + r32) * DM + ncol + 16 + 8 * q + 4 * hh);
        gate = rbf(sigm(rbf(*(const LAS float*)(lds + M_Z + r32 * 4))));
        const bool owner = (idx >> 5) == tt;
        u32x4 yp[22];
        if (owner) {
            for (int it = 0; it < SPIN; ++it) {
                bool ok = true;
#pragma unroll
                for (int i = 0; i < 3; ++i) { const int j = i * 64 + lane; if (j < NSL) ok &= ld_agent(ws + WS_FY + (cb * 192 + j) * 8) == T; }
                if (__all(ok)) break;
                __builtin_amdgcn_s_sleep(16);
            }
            const int cg = st & 31, pg = st >> 5;
#pragma unroll
            for (int i = 0; i < 22; ++i)
                yp[i] = __builtin_amdgcn_raw_buffer_load_b128(rs, (pg + 8 * i < NSL) ? (int)(WS_Y + pg * 2048 + (cb * 128 + cg * 4) * 4) : (int)WS_END, i * 8 * 2048, 16);
        }
        {
            const int rq = lane >> 4, p = lane & 15;
            const unsigned voff = (unsigned)(rq * 1024 + cb * 256 + ((p ^ (rq << 2)) * 16));
            for (int it = 0; it < (1 << 15); ++it) { const unsigned d = lds_ld(lds + M_DONE + (lane & 3) * 4); if (__all(d >= 1u)) break; __builtin_amdgcn_s_sleep(8); }
            asm volatile("" ::: "memory");
            __builtin_amdgcn_s_setprio(3);
            for (int i = sw_; i < 79; i += 4) { const int pc = i < 32 ? i : i < 65 ? i + 24 : i + 49; glds16<false>((const char*)wo + pc * 4096, voff, RFL(lds0 + pc * 1024)); }
            __builtin_amdgcn_s_setprio(0);
            for (int it = 0; it < (1 << 15); ++it) { const unsigned d = lds_ld(lds + M_DONE + (lane & 3) * 4); if (__all(d >= 2u)) break; __builtin_amdgcn_s_sleep(4); }
            asm volatile("" ::: "memory");
            __builtin_amdgcn_s_setprio(3);
            for (int i = sw_; i < 49; i += 4) { const int pc = i < 24 ? i + 32 : i + 65; glds16<false>((const char*)wo + pc * 4096, voff, RFL(lds0 + pc * 1024)); }
            __builtin_amdgcn_s_setprio(0);
        }
        upd[0] = upd[1] = (f32x4){0.f, 0.f, 0.f, 0.f};
        if (owner) {
            const int cg = st & 31, pg = st >> 5;
            f32x4 acc = {0.f, 0.f, 0.f, 0.f};
#pragma unroll
            for (int i = 0; i < 22; ++i) acc += __builtin_bit_cast(f32x4, yp[i]);
            *(LAS f32x4*)(lds + M_YB + (pg * 128 + cg * 4) * 4) = acc;
            asm volatile("s_waitcnt lgkmcnt(0)" ::: "memory");
            lds_add_lane0(lds0 + M_CNT2, 1u);
            lds_wait(lds + M_CNT2, 0xffu, 4u);
            float y = 0.f;
#pragma unroll
            for (int p = 0; p < 8; ++p) y += *(const LAS float*)(lds + M_YB + (p * 128 + nt * 32 + r32) * 4);
            const unsigned o16 = rkey >> 11, rb = (o16 & 0x8000u) ? (o16 & 0x7fffu) : (~o16 & 0xffffu);
            *(LAS float*)(lds + M_UPD + (nt * 32 + r32) * 4) = rbf(rbf(y) * rbf(sigm(__uint_as_float(rb << 16))));
            asm volatile("s_waitcnt lgkmcnt(0)" ::: "memory");
#pragma unroll
            for (int q = 0; q < 2; ++q) upd[q] = *(const LAS f32x4*)(lds + M_UPD + (nt * 32 + 16 + 8 * q + 4 * hh) * 4);
        }
        WAITV(0);
        {
            const char* fp = ws + WS_FO + ((tb * 4 + ((tt & 7) >> 1)) * 32 + r32) * 8;
            if (sw_ == 0) { lds_wait<2>(lds + M_DONE + ((tt & 7) >> 1) * 4, 0xffu, 3u);
                for (int it = 0; it < SPIN; ++it) { if (__all(ld_agent(fp) == T)) break; __builtin_amdgcn_s_sleep(1); }
            }
        }
        if (owner) BAR();
        BAR();
        if (bid == 0 && wid == 4) st_agent_lane0(ws, WS_E, T);
    }
    glds16x4<true>(ws + WS_O + tt * 32768 + wid * 4096, lane * 16, RFL(lds0 + L_O + wid * 4096));

    const LAS char* wp = lds + (4 * hh + q4) * 256 + (((nt ^ q4) * 4 + 2 * g1 + (p4 >> 1)) * 16) + (p4 & 1) * 8 + kh * 65536;
    bf16x8 wf[16];
#pragma unroll
    for (int i = 0; i < 16; ++i) wf[i] = join8(lds_tr(wp + i * 4096), lds_tr(wp + i * 4096 + 2048));
    WAITV(0);
    BAR();
    f32x16 acc = {};
    const LAS char* op = lds + L_O + kh * 16384 + lane * 16;
    bf16x8 of_[16];
#pragma unroll
    for (int i = 0; i < 16; ++i) of_[i] = lds_b128(op + i * 1024);
    __builtin_amdgcn_sched_barrier(0);
#pragma unroll
    for (int i = 0; i < 16; ++i) acc = MFMA32(wf[i], of_[i], acc);
#pragma unroll
    for (int q = 0; q < 2; ++q)
        *(LAS f32x4*)(lds + ((nt * 2 + kh) * 2 + q) * 1024 + lane * 16) =
            kh ? (f32x4){acc[4 * q], acc[4 * q + 1], acc[4 * q + 2], acc[4 * q + 3]} : (f32x4){acc[8 + 4 * q], acc[9 + 4 * q], acc[10 + 4 * q], acc[11 + 4 * q]};
    BAR();
    {
        const int tok = tt * 32 + r32;
        const bool special = tok == idx;
        unsigned pk[4];
#pragma unroll
        for (int q = 0; q < 2; ++q) {
            const f32x4 pz = *(const LAS f32x4*)(lds + ((nt * 2 + (kh ^ 1)) * 2 + q) * 1024 + lane * 16);
            float res[4];
#pragma unroll
            for (int e = 0; e < 4; ++e) {
                float b = (e & 1) ? bf_hi(resid[q][e >> 1]) : bf_lo(resid[q][e >> 1]);
                b += special ? upd[q][e] : 0.f;
                res[e] = fmaf(gate, (kh ? acc[8 + 4 * q + e] : acc[4 * q + e]) + pz[e] - b, b);
            }
            pk[2 * q] = cvtpk(res[0], res[1]); pk[2 * q + 1] = cvtpk(res[2], res[3]);
        }
        auto s0 = __builtin_amdgcn_permlane32_swap(pk[0], pk[2], false, false), s1 = __builtin_amdgcn_permlane32_swap(pk[1], pk[3], false, false);
        *(u32x4*)(out + (size_t)tok * DM + ncol + kh * 16 + hh * 8) = (u32x4){s0[0], s1[0], s0[1], s1[1]};
    }
    asm volatile(".Lfk_end:");
}

extern "C" void kernel_launch(void* const* d_in, const int* in_sizes, int n_in, void* d_out, int out_size, void* d_ws, size_t ws_size, hipStream_t stream) {
    (void)hipFuncSetAttribute((const void*)fused_kernel, hipFuncAttributeMaxDynamicSharedMemorySize, L_BYTES);
    fused_kernel<<<256, 512, L_BYTES, stream>>>((const unsigned short*)d_in[0], (const unsigned short*)d_in[1], (const unsigned short*)d_in[2], (const unsigned short*)d_in[4],
                                                (const unsigned short*)d_in[5], (const unsigned short*)d_in[6], (const unsigned short*)d_in[7], (const unsigned short*)d_in[8],
                                                (char*)d_ws, (unsigned short*)d_out);
}
```
